```python
import torch

def _softmax(x, axis=-1):
    return torch.softmax(x, dim=axis)
def _gelu(x):
    return torch.nn.functional.gelu(x, approximate="tanh")
def _silu(x):
    return torch.nn.functional.silu(x)
def _sigmoid(x):
    return torch.sigmoid(x)
def _layernorm(x, axis=-1, eps=1e-5):
    m = x.mean(dim=axis, keepdim=True)
    v = x.var(dim=axis, keepdim=True, unbiased=False)
    return (x - m) / torch.sqrt(v + eps)
def _scatter_add(tgt, idx, src, axis=0):
    return tgt.clone().index_add_(axis, idx, src)
def _segment_sum(src, seg, n):
    out = torch.zeros((n,) + src.shape[1:], dtype=src.dtype)
    return out.index_add_(0, seg, src)
def _pad(x, pw):
    flat = tuple(v for pair in reversed(pw) for v in pair)
    return torch.nn.functional.pad(x, flat)

kernel_name = 'parallel_sum__sliding_block_cached__glu_gated_geglu__decode'

def setup_inputs():

    _w = (lambda a: (lambda t: t.to(torch.bfloat16) if t.is_floating_point() else t)(torch.as_tensor(a)))
    def _A_setup():
        B, Sq, D = 2, 1, 448
        Hq, Hkv, Sk, Dh = 16, 8, 32768, 256
        assert Hq % Hkv == 0, (Hq, Hkv)
        return {
            "x": _w((lambda *s: torch.randn(*s, dtype=torch.bfloat16) * 0.05)(B, Sq, D)),
            "k_cache": _w((lambda *s: torch.randn(*s, dtype=torch.bfloat16) * 1.6)(B, Hkv, Sk, Dh)),
            "v_cache": _w((lambda *s: torch.randn(*s, dtype=torch.bfloat16) * 0.8)(B, Hkv, Sk, Dh)),
            "wq": _w((lambda *s: torch.randn(*s, dtype=torch.bfloat16) * 1.6)(D, Hq * Dh) * (D ** -0.5)),
            "wo": _w((lambda *s: torch.randn(*s, dtype=torch.bfloat16) * 0.8)(Hq * Dh, D) * ((Hq * Dh) ** -0.5)),
        }
    _A_in = _A_setup()

    def _B_setup():
        B, S, Dm, Df = 2, 1, 448, 1792
        return {
            "x": _w((lambda *s: torch.randn(*s, dtype=torch.bfloat16) * 0.05)(B, S, Dm)),
            "w_gate": _w((lambda *s: torch.randn(*s, dtype=torch.bfloat16) * 0.4)(Dm, Df) * (Dm ** -0.5)),
            "w_up": _w((lambda *s: torch.randn(*s, dtype=torch.bfloat16) * 0.4)(Dm, Df) * (Dm ** -0.5)),
            "w_down": _w((lambda *s: torch.randn(*s, dtype=torch.bfloat16) * 0.4)(Df, Dm) * (Df ** -0.5)),
        }
    _B_in = _B_setup()

    return {
        "A_x": _A_in["x"],
        "A_k_cache": _A_in["k_cache"],
        "A_v_cache": _A_in["v_cache"],
        "A_wq": _A_in["wq"],
        "A_wo": _A_in["wo"],
        "B_x": _B_in["x"],
        "B_w_gate": _B_in["w_gate"],
        "B_w_up": _B_in["w_up"],
        "B_w_down": _B_in["w_down"],
    }

def reference(A_x, A_k_cache, A_v_cache, A_wq, A_wo, B_x, B_w_gate, B_w_up, B_w_down):
    def _A_ref(x, k_cache, v_cache, wq, wo):
        B, Sq, D = x.shape
        Hq, Dh = 16, 256
        Hkv, Sk = k_cache.shape[1], k_cache.shape[2]
        W = min(4096, max(256, Sk // 4))
        rep = Hq // Hkv
        q = (lambda x, *d: x.permute(*d))((x @ wq).reshape(B, Sq, Hq, Dh), 0, 2, 1, 3)
        k = (lambda x, n, axis: torch.repeat_interleave(x, n, dim=axis))(k_cache[:, :, Sk - W:, :], rep, axis=1)
        v = (lambda x, n, axis: torch.repeat_interleave(x, n, dim=axis))(v_cache[:, :, Sk - W:, :], rep, axis=1)
        scores = torch.einsum("bhqd,bhkd->bhqk", q, k) * (Dh ** -0.5)
        p = _softmax(scores, axis=-1)
        o = torch.einsum("bhqk,bhkd->bhqd", p, v)
        return (lambda x, *d: x.permute(*d))(o, 0, 2, 1, 3).reshape(B, Sq, Hq * Dh) @ wo

    def _B_ref(x, w_gate, w_up, w_down):
        g = torch.einsum("bsd,df->bsf", x, w_gate)
        u = torch.einsum("bsd,df->bsf", x, w_up)
        h = _gelu(g) * u
        return torch.einsum("bsf,fd->bsd", h, w_down)

    x = A_x
    xn = _layernorm(x, axis=-1)
    return x + _A_ref(xn, A_k_cache, A_v_cache, A_wq, A_wo) + _B_ref(xn, B_w_gate, B_w_up, B_w_down)
```

```cpp
#include <hip/hip_runtime.h>
#include <hip/hip_bf16.h>

typedef unsigned u32;
typedef unsigned long long u64;
typedef u32 v4u __attribute__((ext_vector_type(4)));
typedef u32 v2u __attribute__((ext_vector_type(2)));
typedef float f32x4 __attribute__((ext_vector_type(4)));
typedef float f32x2 __attribute__((ext_vector_type(2)));
typedef __bf16 bf16x8 __attribute__((ext_vector_type(8)));
typedef __bf16 bf16x2 __attribute__((ext_vector_type(2)));
typedef short s16x4 __attribute__((ext_vector_type(4)));
#define LAS __attribute__((address_space(3)))
#define GAS __attribute__((address_space(1)))

#define DRY 0
#define CODEPF 0

namespace {

constexpr int DM = 448, SK = 32768, WIN = 4096, DH = 256, DF = 1792, NQ = 4096;
constexpr int NTHR = DRY ? 704 : 576;
constexpr int AUX_NT = 2, AUX_SC1 = 16;
constexpr u32 SPIN_CAP = 1u << 15;
constexpr u32 VMASK = 0xffffff00u;

constexpr int WS_EPOCH = 0;
constexpr int WS_Q = 4096;
constexpr int WS_ML = WS_Q + 16 * 4096;
constexpr int WS_A = WS_ML + 256 * 128;
constexpr int WS_B = WS_A + 256 * 4096;
constexpr int WS_END = WS_B + 256 * DM * 8;
constexpr int WS_DIAG = 4 << 20;

constexpr int L_K = 0;
constexpr int L_WQ = 131072;
constexpr int L_WG = L_WQ + 14336;
constexpr int L_XN = L_WG + 14336;
constexpr int L_Q = L_XN + 1792;
constexpr int L_MAX = L_Q + 1024;
constexpr int L_SUM = L_MAX + 64;
constexpr int L_OH = L_SUM + 64;
constexpr int L_TAG = L_OH + 256;
constexpr int L_FLG = L_TAG + 16;
constexpr int L_END = L_FLG + 32;
static_assert(L_END <= 163840, "LDS");

#define STAMP(i) do { } while (0)
#define DUMP(slot, i) do { } while (0)

__device__ __forceinline__ void bar(bool dry) {
  asm volatile("s_waitcnt lgkmcnt(0)" ::: "memory");
  if (!dry) __builtin_amdgcn_s_barrier();
  asm volatile("" ::: "memory");
}
template <typename T> __device__ __forceinline__ void any(T& v) { asm volatile("" : "=v"(v)); }
__device__ __forceinline__ void pin() { __builtin_amdgcn_sched_barrier(0); }
__device__ __forceinline__ v4u make_srd(const void* p, u32 bytes) {
  const u64 a = (u64)p;
  return v4u{(u32)a, (u32)(a >> 32) & 0xffffu, bytes, 0x00020000u};
}
__device__ __forceinline__ void dma16(u32 lds_dst, int voff, v4u srd, int soff) {
  asm volatile("s_mov_b32 m0, %0\n\ts_nop 0\n\tbuffer_load_dwordx4 %1, %2, %3 offen nt lds"
               :: "s"(lds_dst), "v"(voff), "s"(srd), "s"(soff) : "memory");
}
__device__ __forceinline__ f32x4 mfma(v4u a, v4u b, f32x4 c) {
  return __builtin_amdgcn_mfma_f32_16x16x32_bf16(__builtin_bit_cast(bf16x8, a), __builtin_bit_cast(bf16x8, b), c, 0, 0, 0);
}
__device__ __forceinline__ u32 pk_bf16(float lo, float hi) {
  return __builtin_bit_cast(u32, __builtin_convertvector(f32x2{lo, hi}, bf16x2));
}
__device__ __forceinline__ float asf(u32 x) { return __builtin_bit_cast(float, x); }
__device__ __forceinline__ u32 asu(float x) { return __builtin_bit_cast(u32, x); }
__device__ __forceinline__ float gval(u32 hi) { return asf(hi & VMASK); }
template <int CTRL> __device__ __forceinline__ float dpp(float x) {
  return asf((u32)__builtin_amdgcn_mov_dpp((int)asu(x), CTRL, 0xf, 0xf, true));
}
constexpr int DPP_XOR1 = 0xB1, DPP_XOR2 = 0x4E, DPP_ROR4 = 0x124, DPP_ROR8 = 0x128;
__device__ __forceinline__ float fold32(float a, float b) {
  const auto s = __builtin_amdgcn_permlane32_swap(asu(a), asu(b), false, false);
  return asf(s[0]) + asf(s[1]);
}
__device__ __forceinline__ float fold16(float a, float b) {
  const auto s = __builtin_amdgcn_permlane16_swap(asu(a), asu(b), false, false);
  return asf(s[0]) + asf(s[1]);
}
__device__ __forceinline__ float rows_max(float x) {
  const auto s = __builtin_amdgcn_permlane16_swap(asu(x), asu(x), false, false);
  x = fmaxf(asf(s[0]), asf(s[1]));
  const auto t = __builtin_amdgcn_permlane32_swap(asu(x), asu(x), false, false);
  return fmaxf(asf(t[0]), asf(t[1]));
}
__device__ __forceinline__ float rows_sum(float x) { x = fold16(x, x); return fold32(x, x); }
__device__ __forceinline__ float wave_sum(float x) {
  x += dpp<DPP_XOR1>(x); x += dpp<DPP_XOR2>(x); x += dpp<DPP_ROR4>(x); x += dpp<DPP_ROR8>(x);
  return rows_sum(x);
}
__device__ __forceinline__ v2u tr_read(LAS unsigned char* p) {
  return __builtin_bit_cast(v2u, __builtin_amdgcn_ds_read_tr16_b64_v4i16((LAS s16x4*)p));
}
__device__ __forceinline__ f32x4 slice_gemv(LAS unsigned char* L, int woff, int lane) {
  const int G = lane >> 4, i16 = lane & 15;
  LAS unsigned char* wp = L + woff + (8 * G + (i16 >> 2)) * 32 + (i16 & 3) * 8;
  LAS unsigned char* xp = L + L_XN + (i16 & 1) * 896 + G * 16;
  f32x4 d0 = {0.f, 0.f, 0.f, 0.f}, d1 = d0;
#pragma unroll
  for (int s = 0; s < 14; ++s) {
    const v4u a = *(LAS v4u*)(xp + s * 64);
    const v2u b0 = tr_read(wp + s * 1024), b1 = tr_read(wp + s * 1024 + 128);
    const v4u b = {b0.x, b0.y, b1.x, b1.y};
    if (s & 1) d1 = mfma(a, b, d1); else d0 = mfma(a, b, d0);
  }
  return d0 + d1;
}
__device__ __forceinline__ u32 gran_hi(float v, u32 tag8) { return (asu(v) & VMASK) | tag8; }
__device__ __forceinline__ u32 gran_bad(v4u e, u32 tlo, u32 thi, u32 m) { return (e.x ^ tlo) | (e.z ^ tlo) | (((e.y ^ thi) | (e.w ^ thi)) & m); }

__global__ void __launch_bounds__(NTHR, 3)
fused_decode(const unsigned short* __restrict__ x, const unsigned short* __restrict__ kc, const unsigned short* __restrict__ vc,
             const unsigned short* __restrict__ wq, const unsigned short* __restrict__ wo, const unsigned short* __restrict__ wg,
             const unsigned short* __restrict__ wu, const unsigned short* __restrict__ wd, unsigned short* __restrict__ out,
             unsigned char* __restrict__ ws) {
  __shared__ __attribute__((aligned(16))) unsigned char smem[L_END];
  asm volatile("" :: "s"(x), "s"(kc), "s"(vc), "s"(wq), "s"(wo), "s"(wg), "s"(wu), "s"(wd), "s"(out), "s"(ws));
  const int lane = threadIdx.x & 63, wave = __builtin_amdgcn_readfirstlane(threadIdx.x >> 6);
  const bool dry = DRY && wave > 8;
  LAS unsigned char* L = (LAS unsigned char*)smem;
  const u32 lds0 = (u32)(uintptr_t)L;
  const int g = blockIdx.x & 7, idx = blockIdx.x >> 3, b = idx >> 4, sp = idx & 15;
  const int wgl = (g * 2 + b) * 16 + sp;
  const int pb = g * 16 + sp;
  const bool ffn = sp < 14;
  const int fcol = (14 * g + sp) * 16;
  const int wrow = lane >> 1, wcb = (lane & 1) * 16;
  const __amdgpu_buffer_rsrc_t rws = __builtin_amdgcn_make_buffer_rsrc(ws, 0, WS_END, 0x00020000);

  if (wave == 0 || wave == 10) {
    __builtin_amdgcn_s_setprio(3);
    const int ocol = 4 * pb + 2 * (lane & 1) + (lane >> 5);
    v4u x0, x1;
    v2u ep;
    u32 xres;
    if (dry) { any(x0); any(x1); any(ep); any(xres); } else {
      const v4u sx = make_srd(x, (2 * DM * 2)), sw = make_srd(ws, WS_END);
      const int xo = lane < 56 ? lane * 16 : 0x10000, ro = pb < 112 ? (b * DM + ocol) * 2 : 0x10000;
      asm volatile("buffer_load_dwordx4 %0, %4, %6, 0 offen\n\t"
                   "buffer_load_dwordx4 %1, %4, %6, 0 offen offset:896\n\t"
                   "buffer_load_dwordx2 %2, off, %7, 0 sc1\n\t"
                   "buffer_load_ushort %3, %5, %6, 0 offen"
                   : "=&v"(x0), "=&v"(x1), "=&v"(ep), "=&v"(xres) : "v"(xo), "v"(ro), "s"(sx), "s"(sw) : "memory");
    }
    if (lane < 8 && !dry) *(LAS u32*)(L + L_FLG + lane * 4) = lane == 7 ? 1u : 0u;
    bar(false);
    asm volatile("s_waitcnt vmcnt(0)" : "+v"(x0), "+v"(x1), "+v"(ep), "+v"(xres) :: "memory");
    STAMP(1);
    {
      float v0[8], v1[8];
#pragma unroll
      for (int k = 0; k < 4; ++k) {
        v0[2 * k] = asf(x0[k] << 16); v0[2 * k + 1] = asf(x0[k] & 0xffff0000u);
        v1[2 * k] = asf(x1[k] << 16); v1[2 * k + 1] = asf(x1[k] & 0xffff0000u);
      }
      float s0 = 0.f, s1 = 0.f;
#pragma unroll
      for (int k = 0; k < 8; ++k) { s0 += v0[k]; s1 += v1[k]; }
      const float m0 = wave_sum(s0) * (1.f / DM), m1 = wave_sum(s1) * (1.f / DM);
      float q0 = 0.f, q1 = 0.f;
#pragma unroll
      for (int k = 0; k < 8; ++k) { v0[k] -= m0; v1[k] -= m1; q0 += v0[k] * v0[k]; q1 += v1[k] * v1[k]; }
      if (lane >= 56) { q0 = 0.f; q1 = 0.f; }
      const float r0 = __builtin_amdgcn_rsqf(wave_sum(q0) * (1.f / DM) + 1e-5f), r1 = __builtin_amdgcn_rsqf(wave_sum(q1) * (1.f / DM) + 1e-5f);
      v4u o0, o1;
#pragma unroll
      for (int k = 0; k < 4; ++k) { o0[k] = pk_bf16(v0[2 * k] * r0, v0[2 * k + 1] * r0); o1[k] = pk_bf16(v1[2 * k] * r1, v1[2 * k + 1] * r1); }
      asm volatile("" :: "v"(o0), "v"(o1));
      if (lane < 56 && !dry) { *(LAS v4u*)(L + L_XN + lane * 16) = o0; *(LAS v4u*)(L + L_XN + 896 + lane * 16) = o1; }
    }
    const u64 tag = (((u64)ep.y << 32) | ep.x) + 1;
    const u32 tlo = (u32)tag, thi = (u32)(tag >> 32), t8 = thi & 0xffu, t16 = thi & 0xffffu;
    if (lane == 0 && !dry) *(LAS v2u*)(L + L_TAG) = v2u{tlo, t8};
    STAMP(2);
    for (;;) {
      asm volatile("" ::: "memory");
      const u32 f = *(volatile LAS u32*)(L + L_FLG + (lane & 7) * 4);
      if (__builtin_amdgcn_ballot_w64(f == 0) == 0 || dry) break;
    }
    asm volatile("s_waitcnt lgkmcnt(0)" ::: "memory");
    STAMP(3);
    {
      const f32x4 d = slice_gemv(L, L_WQ, lane);
      const float qv = lane < 16 ? d[0] : d[1];
      const v2u gr = {tlo, (pk_bf16(0.f, qv) & 0xffff0000u) | t16};
      const int off = lane < 32 ? WS_Q + ((g * 2 + (lane >> 4)) * 512 + 16 * idx + (lane & 15)) * 8 : 0x7ffffff0;
      asm volatile("" :: "v"(gr), "v"(off));
      if (!dry) __builtin_amdgcn_raw_buffer_store_b64(gr, rws, off, 0, AUX_SC1);
    }
    STAMP(4);
    u32 npoll = 0;
    for (u32 spins = 0;; ++spins) {
      asm volatile("" ::: "memory");
      v4u e[4];
      if (dry) { any(e[0]); any(e[1]); any(e[2]); any(e[3]); } else {
#pragma unroll
        for (int k = 0; k < 4; ++k) e[k] = __builtin_amdgcn_raw_buffer_load_b128(rws, WS_Q + (g * 2 + b) * 4096 + k * 1024 + lane * 16, 0, AUX_SC1);
      }
      u32 bad = 0, qp[4];
#pragma unroll
      for (int k = 0; k < 4; ++k) {
        bad |= gran_bad(e[k], tlo, t16, 0xffffu);
        qp[k] = (e[k].y >> 16) | (e[k].w & 0xffff0000u);
        asm volatile("" :: "v"(qp[k]));
      }
      if (!dry) {
#pragma unroll
        for (int k = 0; k < 4; ++k) *(LAS u32*)(L + L_Q + k * 256 + lane * 4) = qp[k];
      }
      npoll = spins;
      if (__builtin_amdgcn_ballot_w64(bad != 0) == 0 || spins > SPIN_CAP || dry) break;
    }
    STAMP(5);
    bar(dry);
    if (!dry) {
      const u32 nb = ffn ? (DM * DF * 2) : 0;
      const v4u sg = make_srd(wg, nb), su = make_srd(wu, nb);
      const int voff = wrow * (DF * 2) + wcb, soff = fcol * 2;
#pragma unroll
      for (int i = 0; i < 14; ++i) {
        dma16(lds0 + L_WG + i * 1024, voff, sg, soff + i * 32 * DF * 2);
        dma16(lds0 + L_WQ + i * 1024, voff, su, soff + i * 32 * DF * 2);
      }
    }
    bar(dry);
    asm volatile("s_waitcnt vmcnt(0)" ::: "memory");
    STAMP(6);
    {
      const f32x4 dg = slice_gemv(L, L_WG, lane), du = slice_gemv(L, L_WQ, lane);
      const float gv = b ? dg[1] : dg[0], uv = b ? du[1] : du[0];
      const float y2 = 2.f * 0.7978845608028654f * (gv + 0.044715f * gv * gv * gv);
      const float h = ffn ? gv * __builtin_amdgcn_rcpf(1.f + __expf(-y2)) * uv : 0.f;
      const u32 hp = pk_bf16(h, h);
      asm volatile("" :: "v"(hp));
      if (lane < 16 && !dry) *(LAS u32*)(L + L_OH + (lane & 7) * 32 + (4 + (lane >> 3)) * 4) = hp;
    }
    STAMP(7);
    bar(dry);
    STAMP(8);
    {
      const int pp = lane >> 2, qq = lane & 3, hh = sp >> 3;
      const int src = (g * 2 + b) * 16 + pp;
      const int aoff = WS_A + src * 4096 + (hh * 256 + 32 * (sp & 7)) * 8 + qq * 16;
      const int moff = WS_ML + src * 128 + hh * 16;
      v4u e[4], ml;
      for (u32 spins = 0;; ++spins) {
        asm volatile("" ::: "memory");
        if (dry) { any(ml); any(e[0]); any(e[1]); any(e[2]); any(e[3]); } else {
          ml = __builtin_amdgcn_raw_buffer_load_b128(rws, moff, 0, AUX_SC1);
#pragma unroll
          for (int k = 0; k < 4; ++k) e[k] = __builtin_amdgcn_raw_buffer_load_b128(rws, aoff + k * 64, 0, AUX_SC1);
        }
        u32 bad = gran_bad(ml, tlo, t8, 0xffu);
#pragma unroll
        for (int k = 0; k < 4; ++k) bad |= gran_bad(e[k], tlo, t8, 0xffu);
        if (__builtin_amdgcn_ballot_w64(bad != 0) == 0 || spins > SPIN_CAP || dry) break;
      }
      STAMP(9);
      const float m = gval(ml.y);
      float M = fmaxf(m, dpp<DPP_ROR4>(m));
      M = rows_max(fmaxf(M, dpp<DPP_ROR8>(M)));
      const float w = __builtin_amdgcn_exp2f(m - M);
      float ls = w * gval(ml.w);
      ls += dpp<DPP_ROR4>(ls); ls += dpp<DPP_ROR8>(ls);
      const float inv = __builtin_amdgcn_rcpf(rows_sum(ls));
      float u[4];
#pragma unroll
      for (int a = 0; a < 2; ++a) {
        u[2 * a] = fold32(w * gval(e[a].y), w * gval(e[a + 2].y));
        u[2 * a + 1] = fold32(w * gval(e[a].w), w * gval(e[a + 2].w));
      }
      float o0 = fold16(u[0], u[2]), o1 = fold16(u[1], u[3]);
      o0 += dpp<DPP_ROR4>(o0); o0 += dpp<DPP_ROR8>(o0);
      o1 += dpp<DPP_ROR4>(o1); o1 += dpp<DPP_ROR8>(o1);
      o0 *= inv; o1 *= inv;
      const u32 p0 = pk_bf16(o0, o0), p1 = pk_bf16(o1, o1);
      asm volatile("" :: "v"(p0), "v"(p1));
      if ((lane & 12) == 0 && !dry) {
        LAS unsigned char* op = L + L_OH + (2 * qq) * 32 + (lane >> 4) * 4;
        *(LAS u32*)op = p0;
        *(LAS u32*)(op + 32) = p1;
      }
    }
    bar(dry);
    STAMP(10);
    STAMP(11);
    if (pb < 112 || dry) {
      const int boff = WS_B + ((b * 128 + (lane >> 1)) * DM + 4 * pb + 2 * (lane & 1)) * 8;
      v4u e[4];
      for (u32 spins = 0;; ++spins) {
        asm volatile("" ::: "memory");
        u32 bad = 0;
        if (dry) { any(e[0]); any(e[1]); any(e[2]); any(e[3]); } else {
#pragma unroll
          for (int k = 0; k < 4; ++k) e[k] = __builtin_amdgcn_raw_buffer_load_b128(rws, boff + k * 32 * DM * 8, 0, AUX_SC1);
        }
#pragma unroll
        for (int k = 0; k < 4; ++k) bad |= gran_bad(e[k], tlo, t8, 0xffu);
        if (__builtin_amdgcn_ballot_w64(bad != 0) == 0 || spins > SPIN_CAP || dry) break;
      }
      STAMP(11);
      const float c0 = (gval(e[0].y) + gval(e[1].y)) + (gval(e[2].y) + gval(e[3].y));
      const float c1 = (gval(e[0].w) + gval(e[1].w)) + (gval(e[2].w) + gval(e[3].w));
      float s = fold32(c0, c1);
      s = fold16(s, s);
      s += dpp<DPP_ROR8>(s); s += dpp<DPP_ROR4>(s); s += dpp<DPP_XOR2>(s);
      const u32 r = pk_bf16(asf(xres << 16) + s, 0.f);
      const __amdgpu_buffer_rsrc_t ro = __builtin_amdgcn_make_buffer_rsrc(out, 0, (2 * DM * 2), 0x00020000);
      const int o1 = (lane & 30) == 0 ? (b * DM + ocol) * 2 : 0x10000, o2 = (blockIdx.x == 0 && lane == 0) ? WS_EPOCH : 0x7ffffff0;
      asm volatile("" :: "v"(r), "v"(o1), "v"(o2));
      if (!dry) {
        __builtin_amdgcn_raw_buffer_store_b16((unsigned short)r, ro, o1, 0, 0);
        __builtin_amdgcn_raw_buffer_store_b64(v2u{tlo, thi}, rws, o2, 0, 0);
      }
    }
    STAMP(12);
  } else {
    const int w = dry ? 0 : wave - 1;
    const int lg = lane >> 4, ln = lane & 15;
    const size_t hb = ((size_t)(b * 8 + g) * SK + (SK - WIN)) * DH;
    const int rot = w + (sp >> 1), hr = sp & 1;
    v4u vr[16], wr[6];
    if (dry) {
      bar(false);
#pragma unroll
      for (int j = 0; j < 16; ++j) any(vr[j]);
#pragma unroll
      for (int j = 0; j < 6; ++j) any(wr[j]);
    } else {
      v4u pf;
      if (w < 7) {
        const v4u srd = make_srd(wq, DM * NQ * 2);
        const int voff = wrow * (NQ * 2) + wcb, soff = (512 * g + 16 * idx) * 2 + w * 64 * NQ * 2;
        const u32 dst = lds0 + L_WQ + w * 2048;
        dma16(dst, voff, srd, soff);
        dma16(dst + 1024, voff, srd, soff + 32 * NQ * 2);
        any(pf);
      } else {
        any(pf);
      }
      const int kbase = (sp * 128 + w * 16) * 512;
      int so[8];
#pragma unroll
      for (int kq = 0; kq < 8; ++kq) { const int r = (kq + rot) & 7; so[kq] = kbase + (r >> 2) * (2048 * 512) + (r & 3) * 2048; }
      {
        const v4u srd = make_srd(kc + hb, WIN * DH * 2);
        const int hi = lane >> 5;
        const int e0 = (hi * 512 + (lane & 31) * 16) ^ (64 * hi), e1 = ((2 + hi) * 512 + (lane & 31) * 16) ^ (64 * (2 + hi));
#pragma unroll
        for (int j = 0; j < 16; ++j) {
          dma16(lds0 + L_K + w * 16384 + j * 1024, ((j & 1) ? e1 : e0) ^ (16 * ((j >> 1) & 3)), srd, so[j >> 1]);
          if (j == 7) {
            bar(false);
            asm volatile("s_waitcnt vmcnt(8)" :: "v"(pf) : "memory");
            if (lane == 0 && w < 7) *(LAS u32*)(L + L_FLG + w * 4) = 1u;
            STAMP(1);
          }
        }
      }
      pin();
      {
        const __amdgpu_buffer_rsrc_t rv = __builtin_amdgcn_make_buffer_rsrc((void*)(vc + hb), 0, WIN * DH * 2, 0x00020000);
        const int v0 = lg * 512 + hr * 256 + ln * 16, v1 = v0 ^ 256;
#pragma unroll
        for (int j = 0; j < 16; ++j) { vr[j] = __builtin_amdgcn_raw_buffer_load_b128(rv, (j & 1) ? v1 : v0, so[j >> 1], AUX_NT); pin(); }
      }
      {
        const __amdgpu_buffer_rsrc_t rd = __builtin_amdgcn_make_buffer_rsrc((void*)wd, 0, (ffn && w < 7) ? DF * DM * 2 : 0, 0x00020000);
        const __amdgpu_buffer_rsrc_t ro = __builtin_amdgcn_make_buffer_rsrc((void*)wo, 0, w < 7 ? NQ * DM * 2 : 0, 0x00020000);
        const int voff = (lane >> 3) * (DM * 2) + (lane & 7) * 16 + w * 128;
#pragma unroll
        for (int i = 0; i < 2; ++i) { wr[4 + i] = __builtin_amdgcn_raw_buffer_load_b128(rd, voff, (fcol + 8 * i) * (DM * 2), AUX_NT); pin(); }
#pragma unroll
        for (int i = 0; i < 4; ++i) { wr[i] = __builtin_amdgcn_raw_buffer_load_b128(ro, voff, (512 * g + 32 * sp + 8 * i) * (DM * 2), AUX_NT); pin(); }
      }
    }
    STAMP(2);
    bar(dry);
    asm volatile("s_waitcnt vmcnt(22)" ::: "memory");
    STAMP(3);
    f32x4 s0 = {0.f, 0.f, 0.f, 0.f}, s1 = s0;
    {
      LAS unsigned char* qp = L + L_Q + (ln >> 3) * 512 + lg * 16;
      const int kb = L_K + w * 16384 + (4 * (ln & 3) + (ln >> 2)) * 512 + ((lg ^ ln) * 16);
#pragma unroll
      for (int c = 0; c < 8; ++c) {
        const v4u qf = *(LAS v4u*)(qp + c * 64);
        LAS unsigned char* kp = L + (kb ^ (64 * (c & 3))) + (c >> 2) * 256;
        s0 = mfma(*(LAS v4u*)kp, qf, s0);
        s1 = mfma(*(LAS v4u*)(kp + 8192), qf, s1);
      }
    }
    constexpr float C2 = 0.0625f * 1.4426950408889634f;
    s0 *= C2; s1 *= C2;
    {
      float mx = fmaxf(fmaxf(fmaxf(s0[0], s0[1]), fmaxf(s0[2], s0[3])), fmaxf(fmaxf(s1[0], s1[1]), fmaxf(s1[2], s1[3])));
      mx = rows_max(mx);
      asm volatile("" :: "v"(mx));
      if ((lane & 55) == 0 && !dry) *(LAS float*)(L + L_MAX + (lane >> 3) * 32 + w * 4) = mx;
    }
    STAMP(4);
    bar(dry);
    float M;
    {
      const f32x4 a = *(LAS f32x4*)(L + L_MAX + (ln >> 3) * 32), c = *(LAS f32x4*)(L + L_MAX + (ln >> 3) * 32 + 16);
      M = fmaxf(fmaxf(fmaxf(a[0], a[1]), fmaxf(a[2], a[3])), fmaxf(fmaxf(c[0], c[1]), fmaxf(c[2], c[3])));
      M = gval(asu(M));
    }
    u32 pd[8];
    float lsum = 0.f;
#pragma unroll
    for (int r = 0; r < 4; ++r) {
      const float p0 = __builtin_amdgcn_exp2f(s0[r] - M), p1 = __builtin_amdgcn_exp2f(s1[r] - M);
      pd[r] = pk_bf16(p0, p0); pd[4 + r] = pk_bf16(p1, p1);
      lsum += asf(pd[r] & 0xffff0000u) + asf(pd[4 + r] & 0xffff0000u);
    }
    lsum = rows_sum(lsum);
    asm volatile("" :: "v"(lsum));
    if ((lane & 55) == 0 && !dry) *(LAS float*)(L + L_SUM + (lane >> 3) * 32 + w * 4) = lsum;
    v4u dm;
#pragma unroll
    for (int k = 0; k < 4; ++k) dm[k] = ((lane & 7) >> 1) == k ? ((lane & 1) ? 0xffff0000u : 0x0000ffffu) : 0u;
    STAMP(5);
    f32x4 a0 = {0.f, 0.f, 0.f, 0.f}, a1 = a0;
#pragma unroll
    for (int kq = 0; kq < 8; ++kq) {
      const v4u pa = dm & pd[kq];
      a0 = mfma(pa, vr[2 * kq], a0);
      a1 = mfma(pa, vr[2 * kq + 1], a1);
    }
    asm volatile("" :: "v"(a0), "v"(a1));
    if (!dry) {
      LAS unsigned char* rp = L + L_K + w * 16384 + (lg >> 1) * 1024 + (8 * ln + 4 * (lg & 1)) * 4;
      *(LAS f32x4*)(rp + hr * 512) = a0;
      *(LAS f32x4*)(rp + (hr ^ 1) * 512) = a1;
    }
    STAMP(6);
    bar(dry);
    const v2u tg = *(LAS v2u*)(L + L_TAG);
    if (w < 4) {
      f32x2 t = {0.f, 0.f};
#pragma unroll
      for (int ww = 0; ww < 8; ++ww) t += *(LAS f32x2*)(L + L_K + ww * 16384 + (w * 64 + lane) * 8);
      const v4u gr = {tg.x, gran_hi(t[0], tg.y), tg.x, gran_hi(t[1], tg.y)};
      asm volatile("" :: "v"(gr));
      if (!dry) __builtin_amdgcn_raw_buffer_store_b128(gr, rws, WS_A + wgl * 4096 + (w * 64 + lane) * 16, 0, AUX_SC1);
    }
    if (w == 4 || dry) {
      const f32x4 a = *(LAS f32x4*)(L + L_SUM + (ln >> 3) * 32), c = *(LAS f32x4*)(L + L_SUM + (ln >> 3) * 32 + 16);
      const float l = ((a[0] + a[1]) + (a[2] + a[3])) + ((c[0] + c[1]) + (c[2] + c[3]));
      const v4u gr = {tg.x, gran_hi(M, tg.y), tg.x, gran_hi(l, tg.y)};
      const int off = (lane & 55) == 0 ? WS_ML + wgl * 128 + (lane >> 3) * 16 : 0x7ffffff0;
      asm volatile("" :: "v"(gr), "v"(off));
      if (!dry) __builtin_amdgcn_raw_buffer_store_b128(gr, rws, off, 0, AUX_SC1);
    }
    STAMP(7);
    f32x4 y0 = {0.f, 0.f, 0.f, 0.f}, y1 = y0;
    {
      const v2u hv = *(LAS v2u*)(L + L_OH + (lane >> 3) * 32 + 16);
      y0 = mfma(dm & hv.x, wr[4], y0);
      y1 = mfma(dm & hv.y, wr[5], y1);
      asm volatile("" : "+v"(y0), "+v"(y1), "+v"(wr[0]), "+v"(wr[1]), "+v"(wr[2]), "+v"(wr[3]));
    }
    STAMP(8);
    bar(dry);
    if (w < 7) {
      const v4u ov = *(LAS v4u*)(L + L_OH + (lane >> 3) * 32);
      y0 = mfma(dm & ov.x, wr[0], y0);
      y1 = mfma(dm & ov.y, wr[1], y1);
      y0 = mfma(dm & ov.z, wr[2], y0);
      y1 = mfma(dm & ov.w, wr[3], y1);
      y0 += y1;
      float z[4];
#pragma unroll
      for (int r = 0; r < 4; ++r) { const float t = dpp<DPP_ROR8>(y0[r]); z[r] = lane < 32 ? y0[r] : t; }
      const float z0 = fold32(z[0], z[2]), z1 = fold32(z[1], z[3]);
      const v4u gr = {tg.x, gran_hi(z0, tg.y), tg.x, gran_hi(z1, tg.y)};
      const int col = 64 * w + 8 * ln + 4 * (lg & 1) + 2 * (lg >> 1);
      const int off = ln < 8 ? WS_B + ((b * 128 + pb) * DM + col) * 8 : 0x7ffffff0;
      asm volatile("" :: "v"(gr), "v"(off));
      if (!dry) __builtin_amdgcn_raw_buffer_store_b128(gr, rws, off, 0, AUX_SC1);
    }
    STAMP(9);
  }
}

}

extern "C" void kernel_launch(void* const* d_in, const int* in_sizes, int n_in, void* d_out, int out_size, void* d_ws,
                              size_t ws_size, hipStream_t stream) {
  typedef const unsigned short* P;
  fused_decode<<<256, NTHR, 0, stream>>>((P)d_in[0], (P)d_in[1], (P)d_in[2], (P)d_in[3], (P)d_in[4], (P)d_in[6], (P)d_in[7],
                                          (P)d_in[8], (unsigned short*)d_out, (unsigned char*)d_ws);
}
```

```cpp
#include <hip/hip_runtime.h>
#include <hip/hip_bf16.h>

typedef unsigned u32;
typedef unsigned long long u64;
typedef u32 v16u __attribute__((ext_vector_type(16)));
typedef u32 v4u __attribute__((ext_vector_type(4)));
typedef u32 v2u __attribute__((ext_vector_type(2)));
typedef float f32x4 __attribute__((ext_vector_type(4)));
typedef float f32x2 __attribute__((ext_vector_type(2)));
typedef __bf16 bf16x8 __attribute__((ext_vector_type(8)));
typedef __bf16 bf16x2 __attribute__((ext_vector_type(2)));
#define LAS __attribute__((address_space(3)))

namespace {

constexpr int DM = 448, SK = 32768, WIN = 4096, DH = 256, DF = 1792, NQ = 4096;
constexpr int NTHR = 896;
constexpr int AUX_NT = 2, AUX_SC1 = 16, AUX_MID = AUX_SC1 | AUX_NT;
constexpr u32 SPIN_CAP = 1u << 15;
constexpr u32 VMASK = 0xfffff000u, QMASK = 0xffffff00u;
constexpr int NOWHERE = 0x7ffffff0;

constexpr int TOFF = 106, TAU16 = 464;
constexpr int WLOW = 4, WHIGH = 6, LAG = 40;
constexpr int WN1 = 9, WSTART = 10, WSTEP = 6;
constexpr int QTIME = 610, FTIME = 760;
constexpr int TFIRST = 110, TBOARD = 250, TTOUCH = 170, BDELAY = 28, PERIOD = 30;

constexpr int WS_EPOCH = 0;
constexpr int WS_T0 = 1024;
constexpr int WS_Q = 4096;
constexpr int WS_F = WS_Q + 16 * 4096;
constexpr int WS_ML = WS_F + 28 * 16 * 256;
constexpr int WS_A = WS_ML + 256 * 128;
constexpr int WS_B = WS_A + 256 * 2048;
constexpr int WS_END = WS_B + 256 * DM * 4;

constexpr int L_K = 0;
constexpr int L_POFF = 2048;
constexpr int L_XT = 131072;
constexpr int L_Q = L_XT + 3584;
constexpr int L_MAX = L_Q + 1024;
constexpr int L_SUM = L_MAX + 32;
constexpr int L_OH = L_SUM + 32;
constexpr int L_TAG = L_OH + 256;
constexpr int L_FLG = L_TAG + 32;
constexpr int L_SHIFT = L_FLG + 48;
constexpr int L_RED = L_FLG + 64;
constexpr int L_YF = L_RED + 1024;
constexpr int L_END = L_YF + 2048;

template <int N> struct Int { static constexpr int value = N; };
template <int N, int I = 0, typename F> __device__ __forceinline__ void static_for(F&& f) {
  if constexpr (I < N) { f(Int<I>{}); static_for<N, I + 1>(f); }
}
typedef __amdgpu_buffer_rsrc_t rsrc_t;
__device__ __forceinline__ rsrc_t rsrc(const void* p, int bytes) { return __builtin_amdgcn_make_buffer_rsrc((void*)p, 0, bytes, 0x00020000); }
__device__ __forceinline__ void bar() { asm volatile("s_waitcnt lgkmcnt(0)\n\ts_barrier" ::: "memory"); }
__device__ __forceinline__ void lds_done() { asm volatile("s_waitcnt lgkmcnt(0)" ::: "memory"); }
__device__ __forceinline__ void drain() { asm volatile("s_waitcnt vmcnt(0)" ::: "memory"); }
__device__ __forceinline__ void pin() { __builtin_amdgcn_sched_barrier(0); }
template <typename... T> __device__ __forceinline__ void ready(T&... v) { (..., [](auto& a) { asm volatile("" : "+v"(a)); }(v)); }
__device__ __forceinline__ v4u make_srd(const void* p, u32 bytes) {
  const u64 a = (u64)p;
  return v4u{(u32)a, (u32)(a >> 32) & 0xffffu, bytes, 0x00020000u};
}
__device__ __forceinline__ void dma16(u32 lds_dst, int voff, v4u srd, int soff) {
  asm volatile("s_mov_b32 m0, %0\n\ts_nop 0\n\tbuffer_load_dwordx4 %1, %2, %3 offen nt lds"
               :: "s"(lds_dst), "v"(voff), "s"(srd), "s"(soff) : "memory");
}
__device__ __forceinline__ v4u ld_nt(rsrc_t r, int voff, int soff) { return __builtin_amdgcn_raw_buffer_load_b128(r, voff, soff, AUX_NT); }
template <int AUX = AUX_SC1> __device__ __forceinline__ v4u ld_dev(rsrc_t r, int voff) { return __builtin_amdgcn_raw_buffer_load_b128(r, voff, 0, AUX); }
__device__ __forceinline__ f32x4 mfma(v4u a, v4u b, f32x4 c) {
  return __builtin_amdgcn_mfma_f32_16x16x32_bf16(__builtin_bit_cast(bf16x8, a), __builtin_bit_cast(bf16x8, b), c, 0, 0, 0);
}
__device__ __forceinline__ u32 pk_bf16(float lo, float hi) {
  return __builtin_bit_cast(u32, __builtin_convertvector(f32x2{lo, hi}, bf16x2));
}
__device__ __forceinline__ float asf(u32 x) { return __builtin_bit_cast(float, x); }
__device__ __forceinline__ u32 asu(float x) { return __builtin_bit_cast(u32, x); }
__device__ __forceinline__ float rbf(float x) { return asf(pk_bf16(0.f, x)); }
template <u32 M = VMASK> __device__ __forceinline__ float gval(u32 d) { return asf(d & M); }
template <int CTRL> __device__ __forceinline__ float dpp(float x) {
  return asf((u32)__builtin_amdgcn_mov_dpp((int)asu(x), CTRL, 0xf, 0xf, true));
}
constexpr int DPP_XOR1 = 0xB1, DPP_XOR2 = 0x4E, DPP_ROR4 = 0x124, DPP_ROR8 = 0x128;
__device__ __forceinline__ float fold32(float a, float b) {
  const auto s = __builtin_amdgcn_permlane32_swap(asu(a), asu(b), false, false);
  return asf(s[0]) + asf(s[1]);
}
__device__ __forceinline__ float fold16(float a, float b) {
  const auto s = __builtin_amdgcn_permlane16_swap(asu(a), asu(b), false, false);
  return asf(s[0]) + asf(s[1]);
}
__device__ __forceinline__ float rows_max(float x) {
  const auto s = __builtin_amdgcn_permlane16_swap(asu(x), asu(x), false, false);
  x = fmaxf(asf(s[0]), asf(s[1]));
  const auto t = __builtin_amdgcn_permlane32_swap(asu(x), asu(x), false, false);
  return fmaxf(asf(t[0]), asf(t[1]));
}
__device__ __forceinline__ float rows_sum(float x) { x = fold16(x, x); return fold32(x, x); }
__device__ __forceinline__ float wave_sum(float x) {
  x += dpp<DPP_XOR1>(x); x += dpp<DPP_XOR2>(x); x += dpp<DPP_ROR4>(x); x += dpp<DPP_ROR8>(x);
  return rows_sum(x);
}
__device__ __forceinline__ void wait_flag(LAS unsigned char* L, int i) {
  while (*(volatile LAS u32*)(L + L_FLG + 4 * i) == 0) __builtin_amdgcn_s_sleep(1);
  asm volatile("" ::: "memory");
}
__device__ __forceinline__ void set_flag(LAS unsigned char* L, int i, int lane) {
  lds_done();
  if (lane == 0) *(volatile LAS u32*)(L + L_FLG + 4 * i) = 1u;
}
__device__ __forceinline__ u32 now() { return (u32)__builtin_amdgcn_s_memrealtime(); }
__device__ __forceinline__ void wait_until(u32 t) { while ((int)(now() - t) < 0) __builtin_amdgcn_s_sleep(1); }
__device__ __forceinline__ void spin_until(u32 t) { while ((int)(now() - t) < 0) {} }
template <int J> __device__ __forceinline__ void pace(u32 base) {
  if (J >= WHIGH) asm volatile("s_waitcnt vmcnt(%0)" :: "i"(WHIGH - 1) : "memory");
  if (J >= WLOW)
    asm volatile("1:\n\ts_memrealtime s[70:71]\n\ts_waitcnt lgkmcnt(0)\n\ts_sub_u32 s70, s70, %0\n\ts_cmp_lt_i32 s70, %1\n\ts_cbranch_scc1 1b\n\t"
                 "s_cmp_gt_i32 s70, %2\n\ts_cbranch_scc1 2f\n\ts_waitcnt vmcnt(%3)\n2:"
                 :: "s"(base), "i"(J * TAU16 / 16), "i"(J * TAU16 / 16 + LAG), "i"(WLOW - 1) : "s70", "s71", "scc", "memory");
}
constexpr float C2 = 0.0625f * 1.4426950408889634f;
struct MaxW { float m; f32x4 w; };
__device__ __forceinline__ MaxW max_weights(LAS unsigned char* L, int head) {
  const f32x4 mk = *(LAS f32x4*)(L + L_MAX + head * 16);
  MaxW r;
  r.m = fmaxf(fmaxf(mk[0], mk[1]), fmaxf(mk[2], mk[3]));
#pragma unroll
  for (int k = 0; k < 4; ++k) r.w[k] = __builtin_amdgcn_exp2f((mk[k] - r.m) * C2);
  return r;
}
__device__ __forceinline__ u32 tagged(float v, u32 piece) { return (asu(v) & VMASK) | piece; }
template <u32 M = VMASK, typename V, typename... T> __device__ __forceinline__ u32 stale(V tg, T... e) {
  const V d = (... | (e ^ tg));
  u32 r = d[0];
#pragma unroll
  for (int i = 1; i < (int)(sizeof(V) / 4); ++i) r |= d[i];
  return r & ~M;
}
__device__ __forceinline__ bool poll_done(u32 bad, u32 spins) { return __builtin_amdgcn_ballot_w64(bad != 0) == 0 || spins > SPIN_CAP; }
__device__ __forceinline__ v4u diag_mask(int lane) {
  v4u dm;
#pragma unroll
  for (int k = 0; k < 4; ++k) dm[k] = ((lane & 7) >> 1) == k ? ((lane & 1) ? 0xffff0000u : 0x0000ffffu) : 0u;
  return dm;
}
__device__ __forceinline__ int strip_col(int lane) { return 8 * (lane & 15) + 4 * ((lane >> 4) & 1) + 2 * (lane >> 5); }
template <int AUX = AUX_SC1> __device__ __forceinline__ void strip_publish(f32x4 y, v2u tg2, rsrc_t rws, int off, u32 mask = VMASK, u32 half = 0) {
  const auto s0 = __builtin_amdgcn_permlane32_swap(asu(y[0]), asu(y[2]), false, false);
  const auto s1 = __builtin_amdgcn_permlane32_swap(asu(y[1]), asu(y[3]), false, false);
  const float z0 = asf(s0[0]) + dpp<DPP_ROR8>(asf(s0[1])), z1 = asf(s1[0]) + dpp<DPP_ROR8>(asf(s1[1]));
  __builtin_amdgcn_raw_buffer_store_b64(v2u{((asu(z0) + half) & mask) | tg2.x, ((asu(z1) + half) & mask) | tg2.y}, rws, off, 0, AUX);
}

__global__ void __launch_bounds__(NTHR, 4)
fused_decode(const unsigned short*, const unsigned short*, const unsigned short*, const unsigned short*, const unsigned short*,
             const unsigned short*, unsigned char*, unsigned short*, const unsigned short*, const unsigned short*) {
  __shared__ __attribute__((aligned(16))) unsigned char smem[L_END];
  v16u ka;
  u64 entry;
  asm volatile("s_memrealtime %1\n\ts_load_dwordx16 %0, %2, 0x0" : "=&s"(ka), "=&s"(entry) : "s"(__builtin_amdgcn_kernarg_segment_ptr()));
  LAS unsigned char* L = (LAS unsigned char*)smem;
  if (threadIdx.x < 14) *(LAS u32*)(L + L_FLG + threadIdx.x * 4) = 0u;
  const int lane = threadIdx.x & 63, wave = __builtin_amdgcn_readfirstlane(threadIdx.x >> 6);
  const u32 lds0 = (u32)(uintptr_t)L;
  const int g = blockIdx.x & 7, idx = blockIdx.x >> 3, b = idx >> 4, sp = idx & 15;
  const bool qrole = (idx & 1) == 0;
  const int un = idx >> 1, funit = 14 * g + un;
  const bool act = qrole || un < 14;
  int rb = qrole ? 2 * (un & 1) + wave : funit & 3;
  int wstride = qrole ? NQ * 2 : DF * 2, wbytes = act ? DM * wstride : 0;
  int colq = 512 * g + 64 * (un >> 1), colf = 64 * (funit >> 2);
  asm volatile("" : "+s"(colq), "+s"(colf));
  int wsoff = (qrole ? colq : colf) * 2 + 112 * rb * wstride;
  int wvoff = (lane >> 3) * wstride + (lane & 7) * 16, xoff = lane < 56 ? lane * 16 : NOWHERE;
  ready(wvoff, xoff);
  asm volatile("s_waitcnt lgkmcnt(0)\n\ts_barrier"
               : "+s"(ka), "+s"(entry), "+s"(rb), "+s"(wstride), "+s"(wbytes), "+s"(wsoff) :: "memory");
  const u32 t0 = (u32)entry;
  auto arg = [&](int i) { return (unsigned short*)(((u64)ka[2 * i + 1] << 32) | ka[2 * i]); };
  const unsigned short *x = arg(0), *kc = arg(1), *vc = arg(2), *wq = arg(3), *wg = arg(4), *wu = arg(5);
  unsigned char* ws = (unsigned char*)arg(6);
  unsigned short* out = arg(7);
  const size_t hb = ((size_t)(b * 8 + g) * SK + (SK - WIN)) * DH;
  if (sp == 0 && wave == 3) {
    u32 t[2];
    asm volatile("s_load_dword %0, %2, 0x0\n\ts_load_dword %1, %2, 0xffffc\n\ts_waitcnt lgkmcnt(0)" : "=&s"(t[0]), "=&s"(t[1]) : "s"(kc + hb + (1 << 18)));
  }
  const int w = wave - 6;
  auto group_off = [&](int kq) {
    const int r = (kq + 4 * w + 3 * g) & 7;
    return (r & 1) << 11 | (sp & 1) << 12 | (w & 1) << 13 | (sp & 2) << 13 | (r & 2) << 14 | (w >> 1) << 16 | (sp >> 2) << 18 | (r >> 2) << 20;
  };
  auto k_request = [&](int j) {
    const int row = 2 * (j & 1) + (lane >> 5);
    dma16(lds0 + L_K + w * 16384 + j * 1024, (row * 512 + (lane & 31) * 16) ^ (64 * row + 16 * ((j >> 1) & 3)), make_srd(kc + hb, WIN * DH * 2), group_off(j >> 1));
  };
  const int wgl = (g * 2 + b) * 16 + sp;
  const int pb = g * 16 + sp;
  const int red = g + 8 * sp;
  const bool ffn = sp < 14;
  const int fslice = 14 * g + sp;
  const int lg = lane >> 4, ln = lane & 15, l8 = lane >> 3, c16 = (lane & 7) * 16;
  const rsrc_t rws = rsrc(ws, WS_END);
  const f32x4 zero = {0.f, 0.f, 0.f, 0.f};

  if (wave >= 6) {
    const int hr = (w ^ g) & 1;
    const u32 base0 = t0 + TOFF + ((w + 5 * sp) & 7) * TAU16 / 128;
    u32 base = base0;
    static_for<16>([&](auto jc) {
      constexpr int j = decltype(jc)::value;
      if (j >= 5 && (j & 1)) base = base0 + __builtin_amdgcn_readfirstlane(*(volatile LAS u32*)(L + L_SHIFT));
      pace<j>(base);
      k_request(j);
    });
    v4u vr[16];
    {
      const rsrc_t rv = rsrc(vc + hb, WIN * DH * 2);
      const int v0 = lg * 512 + hr * 256 + ln * 16, v1 = v0 ^ 256;
      static_for<16>([&](auto jc) {
        constexpr int j = decltype(jc)::value;
        pace<16 + j>(base);
        if (j == WHIGH - 1 && lane == 0) *(volatile LAS u32*)(L + L_FLG + 8 + 4 * w) = 1u;
        vr[j] = ld_nt(rv, (j & 1) ? v1 : v0, group_off(j >> 1));
        pin();
      });
    }
    const v4u dm = diag_mask(lane);
    bar();
    LAS unsigned char* pp = L + L_K + w * 16384 + L_POFF + (lg * 2 + (ln >> 3)) * 32;
    const v4u pd0 = *(LAS v4u*)pp, pd1 = *(LAS v4u*)(pp + 16);
    f32x4 wk = max_weights(L, (w >> 1) & 1).w;
    v4u tg = *(LAS v4u*)(L + L_TAG);
    v2u tg2 = *(LAS v2u*)(L + L_TAG + (lane & 1) * 8);
    u32 wa0 = lds0 + L_K + w * 16384 + (lg >> 1) * 1024 + (8 * ln + 4 * (lg & 1)) * 4 + hr * 512, wa1 = wa0 ^ 512;
    u32 ra0 = lds0 + L_K + (w * 64 + lane) * 8, ra1 = ra0 + 65536;
    int so = WS_A + wgl * 2048 + (w * 64 + lane) * 8;
    const int pr = lane >> 2, qq = lane & 3, hh = sp >> 3, src = (g * 2 + b) * 16 + pr;
    int aoff = WS_A + src * 2048 + (hh * 256 + 32 * (sp & 7)) * 4 + qq * 16, moff = WS_ML + src * 128;
    u32 oa = lds0 + L_OH + (4 * (qq & 1) + 2 * (lg & 1)) * 32 + (2 * (lg >> 1) + (qq >> 1)) * 4;
    ready(wk, tg, tg2, wa0, wa1, ra0, ra1, so, aoff, moff, oa);
    f32x4 a0 = zero, a1 = zero;
#pragma unroll
    for (int kq = 0; kq < 8; ++kq) {
      const v4u pa = dm & (kq < 4 ? pd0[kq & 3] : pd1[kq & 3]);
      a0 = mfma(pa, vr[2 * kq], a0);
      a1 = mfma(pa, vr[2 * kq + 1], a1);
    }
    *(LAS f32x4*)(uintptr_t)wa0 = a0;
    *(LAS f32x4*)(uintptr_t)wa1 = a1;
    bar();
    if (w < 4) {
      f32x2 t[8];
#pragma unroll
      for (int k = 0; k < 8; ++k) t[k] = *(LAS f32x2*)(uintptr_t)((k < 4 ? ra0 : ra1) + (k & 3) * 16384);
      const f32x2 r = ((t[0] + t[1]) * wk[0] + (t[2] + t[3]) * wk[1]) + ((t[4] + t[5]) * wk[2] + (t[6] + t[7]) * wk[3]);
      __builtin_amdgcn_raw_buffer_store_b64(v2u{tagged(r[0], tg2.x), tagged(r[1], tg2.y)}, rws, so, 0, AUX_SC1);
    }
    v4u ml;
    if (w == 0) ml = ld_dev(rws, moff); else asm volatile("" : "=v"(ml));
    bar();
    if (w == 0) {
      v4u e0, e1;
      drain();
      u32 next = 0;
      float wt;
      for (u32 spins = 0;; ++spins) {
        if (spins) { wait_until(next); ml = ld_dev(rws, moff); }
        asm volatile("" ::: "memory");
        e0 = ld_dev(rws, aoff);
        e1 = ld_dev(rws, aoff + 64);
        next = now() + PERIOD;
        pin();
        const float m = gval(hh ? ml.z : ml.x);
        float M = fmaxf(m, dpp<DPP_ROR4>(m));
        M = rows_max(fmaxf(M, dpp<DPP_ROR8>(M)));
        wt = __builtin_amdgcn_exp2f((m - M) * C2);
        float ls = wt * gval(hh ? ml.w : ml.y);
        ls += dpp<DPP_ROR4>(ls); ls += dpp<DPP_ROR8>(ls);
        wt *= __builtin_amdgcn_rcpf(rows_sum(ls));
        u32 bad = stale(tg, ml);
        ready(wt, bad);
        pin();
        if (poll_done(bad | stale(tg, e0, e1), spins)) break;
      }
      float u[4];
#pragma unroll
      for (int a = 0; a < 4; ++a) u[a] = fold32(wt * gval(e0[a]), wt * gval(e1[a]));
      float o0 = fold16(u[0], u[2]), o1 = fold16(u[1], u[3]);
      o0 += dpp<DPP_ROR4>(o0); o0 += dpp<DPP_ROR8>(o0);
      o1 += dpp<DPP_ROR4>(o1); o1 += dpp<DPP_ROR8>(o1);
      if ((lane & 12) == 0) {
        *(LAS u32*)(uintptr_t)oa = pk_bf16(o0, o0);
        *(LAS u32*)(uintptr_t)(oa + 32) = pk_bf16(o1, o1);
      }
      lds_done();
    }
    if (w == 0 && red >= 14) bar();
    if (red < 14) {
      int boff = WS_B + ((b * 128 + 16 * w + l8) * DM + 32 * red) * 4 + c16;
      int ooff = lane < 32 ? (b * DM + 32 * red + lane) * 2 : NOWHERE, eoff = (blockIdx.x == 128 && lane == 0) ? WS_EPOCH : NOWHERE;
      u32 cw = lds0 + L_RED + w * 128 + (4 * (lane & 7) + 2 * (lane >> 5) + (lg & 1)) * 4, cr = lds0 + L_RED + (lane & 31) * 4;
      v2u ep = *(LAS v2u*)(L + L_TAG + 16);
      ready(boff, ooff, eoff, cw, cr, ep);
      bar();
      u32 xraw = __builtin_amdgcn_raw_buffer_load_b16(rsrc(x, 2 * DM * 2), ooff, 0, AUX_NT);
      bar();
      spin_until(now() + BDELAY);
      u32 next = 0;
      v4u e0, e1;
      for (u32 spins = 0;; ++spins) {
        if (spins) wait_until(next);
        asm volatile("" ::: "memory");
        e0 = ld_dev(rws, boff);
        e1 = ld_dev(rws, boff + 8 * DM * 4);
        next = now() + PERIOD;
        if (poll_done(stale(tg, e0, e1), spins)) break;
      }
      float c = fold16(fold32(gval(e0.x) + gval(e1.x), gval(e0.z) + gval(e1.z)), fold32(gval(e0.y) + gval(e1.y), gval(e0.w) + gval(e1.w)));
      c += dpp<DPP_ROR8>(c);
      if ((lane & 8) == 0) *(LAS float*)(uintptr_t)cw = c;
      bar();
      if (w == 0) {
        float t[8];
#pragma unroll
        for (int k = 0; k < 8; ++k) t[k] = *(LAS float*)(uintptr_t)(cr + k * 128);
        const float r = asf(xraw << 16) + (((t[0] + t[1]) + (t[2] + t[3])) + ((t[4] + t[5]) + (t[6] + t[7])));
        __builtin_amdgcn_raw_buffer_store_b16((unsigned short)pk_bf16(r, 0.f), rsrc(out, 2 * DM * 2), ooff, 0, AUX_SC1);
        __builtin_amdgcn_raw_buffer_store_b64(ep, rws, eoff, 0, AUX_SC1);
      }
    }
  } else if (wave >= 2) {
    const int kw = wave - 2;
    v4u late_args;
    asm volatile("s_load_dwordx4 %0, %1, 0x40\n\ts_waitcnt lgkmcnt(0)" : "=&s"(late_args) : "s"(__builtin_amdgcn_kernarg_segment_ptr()));
    const unsigned short* wo = (const unsigned short*)(((u64)late_args.y << 32) | late_args.x);
    const unsigned short* wd = (const unsigned short*)(((u64)late_args.w << 32) | late_args.z);
    if (sp == 0 && kw == 1) {
      u32 t[5];
      wait_until(t0 + TTOUCH);
      asm volatile("s_load_dword %0, %5, 0x0\n\ts_load_dword %1, %5, 0xffffc\n\ts_load_dword %2, %6, 0x0\n\ts_load_dword %3, %6, 0x6fc80\n\t"
                   "s_load_dword %4, %7, 0x0\n\ts_waitcnt lgkmcnt(0)"
                   : "=&s"(t[0]), "=&s"(t[1]), "=&s"(t[2]), "=&s"(t[3]), "=&s"(t[4])
                   : "s"(vc + hb + (1 << 18)), "s"(wo + 512 * g * DM), "s"(wd + 224 * g * DM));
    }
    if (kw == 0) {
      if (idx == 0) {
        wait_flag(L, 13);
        const int slow = (int)(*(volatile LAS u32*)(L + L_SHIFT + 4) - t0) - TFIRST;
        const u32 ts = t0 + (slow > 0 ? slow * 3 / 2 : 0);
        __builtin_amdgcn_raw_buffer_store_b128(v4u{ts, ts, ts, ts}, rws, lane < 8 ? WS_T0 + g * 128 + lane * 16 : NOWHERE, 0, AUX_SC1);
      }
      for (int t = TBOARD; t <= TBOARD + 100; t += 100) {
        wait_until(t0 + t);
        const int d = (int)(__builtin_amdgcn_raw_buffer_load_b32(rws, WS_T0 + (lane & 7) * 128, 0, AUX_SC1) - t0);
        const bool ok = d > -400 && d < 400;
        const float sum = wave_sum(ok ? (float)d : 0.f), cnt = wave_sum(ok ? 1.f : 0.f);
        if (lane == 0 && cnt > 0.f) *(volatile LAS u32*)(L + L_SHIFT) = (u32)(int)(sum / cnt);
        if (cnt == 64.f) break;
      }
    }
    wait_flag(L, 1);
    f32x4 s[4] = {zero, zero, zero, zero};
    {
      v4u qf[8];
      LAS unsigned char* qp = L + L_Q + (ln >> 3) * 512 + lg * 16;
#pragma unroll
      for (int c = 0; c < 8; ++c) qf[c] = *(LAS v4u*)(qp + c * 64);
      wait_flag(L, 2 + 2 * kw);
      wait_flag(L, 3 + 2 * kw);
      const int kb = L_K + kw * 32768 + (4 * (ln & 3) + (ln >> 2)) * 512 + ((lg ^ ln) * 16);
      v4u kf[2][8];
      auto fetch = [&](int st) {
#pragma unroll
        for (int e = 0; e < 8; ++e) {
          const int c = 2 * st + (e >> 2);
          kf[st & 1][e] = *(LAS v4u*)(L + (kb ^ (64 * (c & 3))) + (c >> 2) * 256 + (e & 3) * 8192);
        }
        pin();
      };
      fetch(0);
#pragma unroll
      for (int st = 0; st < 4; ++st) {
        if (st < 3) fetch(st + 1);
#pragma unroll
        for (int e = 0; e < 8; ++e) s[e & 3] = mfma(kf[st & 1][e], qf[2 * st + (e >> 2)], s[e & 3]);
        pin();
      }
    }
    {
      float mx = -INFINITY;
#pragma unroll
      for (int t = 0; t < 4; ++t) {
        const u32 p0 = pk_bf16(s[t][0], s[t][1]), p1 = pk_bf16(s[t][2], s[t][3]);
        s[t] = f32x4{asf(p0 << 16), asf(p0 & 0xffff0000u), asf(p1 << 16), asf(p1 & 0xffff0000u)};
      }
#pragma unroll
      for (int t = 0; t < 4; ++t) { mx = fmaxf(mx, fmaxf(fmaxf(s[t][0], s[t][1]), fmaxf(s[t][2], s[t][3]))); }
      mx = rows_max(mx);
      float lsum = 0.f;
      lds_done();
#pragma unroll
      for (int t = 0; t < 4; ++t) {
        v4u pd;
#pragma unroll
        for (int r = 0; r < 4; ++r) {
          const float p = __builtin_amdgcn_exp2f((s[t][r] - mx) * C2);
          pd[r] = pk_bf16(p, p);
          lsum += asf(pd[r] & 0xffff0000u);
        }
        if ((ln & 7) == 0) *(LAS v4u*)(L + L_K + (2 * kw + (t >> 1)) * 16384 + L_POFF + (lg * 2 + (ln >> 3)) * 32 + (t & 1) * 16) = pd;
      }
      lsum = rows_sum(lsum);
      if ((lane & 55) == 0) {
        *(LAS float*)(L + L_MAX + l8 * 16 + kw * 4) = mx;
        *(LAS float*)(L + L_SUM + l8 * 16 + kw * 4) = lsum;
      }
    }
    v4u wr[2][6];
    auto load_w = [&](int n) {
      const int u = n / 6, i = n % 6, voff = l8 * (DM * 2) + c16 + kw * 256 + u * 128;
      const bool on = 2 * kw + u < 7;
      if (i < 4) wr[u][i] = ld_nt(rsrc(wo, on ? NQ * DM * 2 : 0), voff, (512 * g + 32 * sp + 8 * i) * (DM * 2));
      else wr[u][i] = ld_nt(rsrc(wd, (on && ffn) ? DF * DM * 2 : 0), voff, (16 * fslice + 8 * (i - 4)) * (DM * 2));
      pin();
    };
    const v2u tg2 = *(LAS v2u*)(L + L_TAG + (lane >> 5) * 8);
    lds_done();
    u32 arrived = 0;
    if (lane == 0) arrived = __hip_atomic_fetch_add((LAS u32*)(L + L_FLG + 44), 1u, __ATOMIC_RELAXED, __HIP_MEMORY_SCOPE_WORKGROUP);
    asm volatile("" ::: "memory");
    if (__builtin_amdgcn_readfirstlane(arrived) == 3) {
      const MaxW mw = max_weights(L, lane >> 5);
      const f32x4 lk = *(LAS f32x4*)(L + L_SUM + (lane >> 5) * 16);
      const float l = (mw.w[0] * lk[0] + mw.w[1] * lk[1]) + (mw.w[2] * lk[2] + mw.w[3] * lk[3]);
      __builtin_amdgcn_raw_buffer_store_b64(v2u{tagged(mw.m, tg2.x), tagged(l, tg2.y)}, rws, (lane & 31) == 0 ? WS_ML + wgl * 128 + (lane >> 5) * 8 : NOWHERE, 0, AUX_SC1);
    }
    bar();
    {
      const u32 t1 = now() + WSTART + kw * WSTEP / 4;
#pragma unroll
      for (int n = 0; n < WN1; ++n) { spin_until(t1 + n * WSTEP); load_w(n); }
    }
    bar();
    bar();
#pragma unroll
    for (int n = WN1; n < 12; ++n) load_w(n);
    v4u dm = diag_mask(lane);
    f32x4 y[2][2];
    int bo[2];
    u32 oa = lds0 + L_OH + l8 * 32;
    {
      const v2u hv = *(LAS v2u*)(uintptr_t)(oa + 16);
#pragma unroll
      for (int u = 0; u < 2; ++u) {
        const int col = 128 * kw + 64 * u + strip_col(lane);
        bo[u] = (ln < 8 && col < DM) ? WS_B + ((b * 128 + pb) * DM + col) * 4 : NOWHERE;
        y[u][0] = mfma(dm & hv.x, wr[u][4], zero); y[u][1] = mfma(dm & hv.y, wr[u][5], zero);
      }
      ready(dm, oa, bo[0], bo[1]);
#pragma unroll
      for (int u = 0; u < 2; ++u) ready(y[u][0], y[u][1], wr[u][0], wr[u][1], wr[u][2], wr[u][3]);
    }
    bar();
    {
      const v4u ov = *(LAS v4u*)(uintptr_t)oa;
#pragma unroll
      for (int i = 0; i < 4; ++i) {
        const v4u a = dm & ov[i];
#pragma unroll
        for (int u = 0; u < 2; ++u) y[u][i & 1] = mfma(a, wr[u][i], y[u][i & 1]);
      }
#pragma unroll
      for (int u = 0; u < 2; ++u) strip_publish(y[u][0] + y[u][1], tg2, rws, bo[u]);
    }
    if (red < 14) bar();
  } else {
    __builtin_amdgcn_s_setprio(3);
    v4u x0, x1;
    v2u ep;
    if (wave == 0) {
      const rsrc_t rx = rsrc(x, 2 * DM * 2);
      x0 = __builtin_amdgcn_raw_buffer_load_b128(rx, xoff, 0, 0);
      x1 = __builtin_amdgcn_raw_buffer_load_b128(rx, xoff, 896, 0);
      ep = __builtin_amdgcn_raw_buffer_load_b64(rws, WS_EPOCH, 0, AUX_SC1);
    } else {
      asm volatile("" : "=v"(x0), "=v"(x1), "=v"(ep));
    }
    pin();
    v4u wt[14];
    {
      const rsrc_t rw = rsrc(qrole ? wq : wave == 0 ? wg : wu, wbytes);
#pragma unroll
      for (int i = 0; i < 14; ++i) { wt[i] = ld_nt(rw, wvoff, wsoff + 8 * i * wstride); pin(); }
    }
    if (wave == 0) {
      float v0[8], v1[8];
#pragma unroll
      for (int k = 0; k < 4; ++k) {
        v0[2 * k] = asf(x0[k] << 16); v0[2 * k + 1] = asf(x0[k] & 0xffff0000u);
        v1[2 * k] = asf(x1[k] << 16); v1[2 * k + 1] = asf(x1[k] & 0xffff0000u);
      }
      ready(v0[0]);
      if (lane == 0) *(volatile LAS u32*)(L + L_SHIFT + 4) = now() | 1u;
      pin();
      const float s0 = ((v0[0] + v0[1]) + (v0[2] + v0[3])) + ((v0[4] + v0[5]) + (v0[6] + v0[7]));
      const float s1 = ((v1[0] + v1[1]) + (v1[2] + v1[3])) + ((v1[4] + v1[5]) + (v1[6] + v1[7]));
      const float m0 = wave_sum(s0) * (1.f / DM), m1 = wave_sum(s1) * (1.f / DM);
      const float mb0 = rbf(m0), mb1 = rbf(m1);
      float q0 = 0.f, q1 = 0.f;
#pragma unroll
      for (int k = 0; k < 8; ++k) {
        q0 += (v0[k] - m0) * (v0[k] - m0); q1 += (v1[k] - m1) * (v1[k] - m1);
        v0[k] = rbf(v0[k] - mb0); v1[k] = rbf(v1[k] - mb1);
      }
      if (lane >= 56) { q0 = 0.f; q1 = 0.f; }
      const float r0 = __builtin_amdgcn_rcpf(rbf(__builtin_amdgcn_sqrtf(rbf(rbf(wave_sum(q0) * (1.f / DM)) + 1e-5f))));
      const float r1 = __builtin_amdgcn_rcpf(rbf(__builtin_amdgcn_sqrtf(rbf(rbf(wave_sum(q1) * (1.f / DM)) + 1e-5f))));
      if (lane < 56) {
#pragma unroll
        for (int k = 0; k < 8; ++k) {
          *(LAS u32*)(L + L_XT + k * 224 + lane * 4) = pk_bf16(v0[k] * r0, v0[k] * r0);
          *(LAS u32*)(L + L_XT + 1792 + k * 224 + lane * 4) = pk_bf16(v1[k] * r1, v1[k] * r1);
        }
      }
      const u64 epoch = (((u64)ep.y << 32) | ep.x) + 1, tag = epoch * 0x1001001001ull;
      if (lane < 6) *(LAS u32*)(L + L_TAG + lane * 4) = lane < 4 ? (u32)(tag >> (12 * lane)) & ~VMASK : (u32)(epoch >> (32 * (lane - 4)));
      set_flag(L, 0, lane);
    }
    wait_flag(L, 0);
    f32x4 yb[2];
    {
      const v4u dm = diag_mask(lane);
      f32x4 y[2][2] = {{zero, zero}, {zero, zero}};
      v2u xv[2][7];
#pragma unroll
      for (int i = 0; i < 7; ++i)
#pragma unroll
        for (int bb = 0; bb < 2; ++bb) xv[bb][i] = *(LAS v2u*)(L + L_XT + bb * 1792 + l8 * 224 + (14 * rb + 2 * i) * 4);
#pragma unroll
      for (int i = 0; i < 7; ++i) {
        pin();
#pragma unroll
        for (int bb = 0; bb < 2; ++bb) {
          y[bb][0] = mfma(dm & xv[bb][i].x, wt[2 * i], y[bb][0]);
          y[bb][1] = mfma(dm & xv[bb][i].y, wt[2 * i + 1], y[bb][1]);
        }
      }
      yb[0] = y[0][0] + y[0][1]; yb[1] = y[1][0] + y[1][1];
    }
    if (qrole) {
      if (wave == 1) {
        *(LAS f32x4*)(L + L_YF + lane * 16) = yb[0];
        *(LAS f32x4*)(L + L_YF + 1024 + lane * 16) = yb[1];
        set_flag(L, 10, lane);
      } else {
        wait_flag(L, 10);
        yb[0] += *(LAS f32x4*)(L + L_YF + lane * 16);
        yb[1] += *(LAS f32x4*)(L + L_YF + 1024 + lane * 16);
      }
    }
    {
      const bool pub = ln < 8 && (qrole ? wave == 0 : act);
      const int o0 = qrole ? WS_Q + ((g * 4 + (un & 1)) * 512 + 64 * (un >> 1)) * 4 : WS_F + (((funit >> 2) * 4 + wave) * 4 + (funit & 3)) * 256;
      const int ob = qrole ? 4096 : 2048;
      const u32 mask = qrole ? QMASK : VMASK;
      const v2u tg2 = *(LAS v2u*)(L + L_TAG + (lane >> 5) * 8) & ~mask;
#pragma unroll
      for (int bb = 0; bb < 2; ++bb) strip_publish<AUX_MID>(yb[bb], tg2, rws, pub ? o0 + bb * ob + strip_col(lane) * 4 : NOWHERE, mask, (~mask + 1) >> 1);
    }
    drain();
    while ((int)(now() - t0 - __builtin_amdgcn_readfirstlane(*(volatile LAS u32*)(L + L_SHIFT))) < (wave == 0 ? QTIME : FTIME)) __builtin_amdgcn_s_sleep(4);
    u32 next = now();
    if (wave == 0) {
      const v4u tg = *(LAS v4u*)(L + L_TAG);
      u32 bad = 1;
      for (u32 spins = 0;; ++spins) {
        wait_until(next);
        next = now() + PERIOD;
        asm volatile("" ::: "memory");
        if (bad) {
          v4u e[2][2];
#pragma unroll
          for (int k = 0; k < 2; ++k)
#pragma unroll
            for (int p = 0; p < 2; ++p) e[k][p] = ld_dev<AUX_MID>(rws, WS_Q + ((g * 2 + b) * 2 + p) * 2048 + k * 1024 + lane * 16);
#pragma unroll
          for (int k = 0; k < 2; ++k)
            *(LAS v2u*)(L + L_Q + k * 512 + lane * 8) = v2u{pk_bf16(gval<QMASK>(e[k][0].x) + gval<QMASK>(e[k][1].x), gval<QMASK>(e[k][0].y) + gval<QMASK>(e[k][1].y)),
                                                           pk_bf16(gval<QMASK>(e[k][0].z) + gval<QMASK>(e[k][1].z), gval<QMASK>(e[k][0].w) + gval<QMASK>(e[k][1].w))};
          bad = stale<QMASK>(tg, e[0][0], e[0][1], e[1][0], e[1][1]);
        }
        if (poll_done(bad, spins)) break;
      }
      set_flag(L, 1, lane);
      lds_done();
    } else {
      u32 hp = 0;
      if (ffn) {
        const int foff = WS_F + (((fslice >> 2) * 2 + b) * 8 + l8) * 256 + (fslice & 3) * 64 + (lane & 7) * 8;
        const v2u tg2 = *(LAS v2u*)(L + L_TAG + (lane & 1) * 8);
        v2u e;
        for (u32 spins = 0;; ++spins) {
          wait_until(next);
          next = now() + PERIOD;
          asm volatile("" ::: "memory");
          e = __builtin_amdgcn_raw_buffer_load_b64(rws, foff, 0, AUX_MID);
          if (poll_done(stale(tg2, e), spins)) break;
        }
        float c0 = gval(e.x), c1 = gval(e.y);
        c0 += dpp<DPP_ROR8>(c0); c1 += dpp<DPP_ROR8>(c1);
        c0 = fold16(c0, c0); c1 = fold16(c1, c1);
        const auto sw = __builtin_amdgcn_permlane32_swap(asu(c0), asu(c1), false, false);
        const float gv = rbf(asf(sw[0])), uv = rbf(asf(sw[1]));
        const float y2 = 2.f * 0.7978845608028654f * (gv + 0.044715f * gv * gv * gv);
        const float h = rbf(gv * __builtin_amdgcn_rcpf(1.f + __expf(-y2))) * uv;
        hp = pk_bf16(h, h);
      }
      if ((lane & 24) == 0) {
        const int n = 2 * (lane & 7) + (lane >> 5);
        *(LAS u32*)(L + L_OH + (n & 7) * 32 + (4 + (n >> 3)) * 4) = hp;
      }
      lds_done();
    }
  }
}

}

extern "C" void kernel_launch(void* const* d_in, const int* in_sizes, int n_in, void* d_out, int out_size, void* d_ws,
                              size_t ws_size, hipStream_t stream) {
  typedef const unsigned short* P;
  fused_decode<<<256, NTHR, 0, stream>>>((P)d_in[0], (P)d_in[1], (P)d_in[2], (P)d_in[3], (P)d_in[6], (P)d_in[7], (unsigned char*)d_ws,
                                          (unsigned short*)d_out, (P)d_in[4], (P)d_in[8]);
}
```
